```python
import torch

def _softmax(x, axis=-1):
    return torch.softmax(x, dim=axis)
def _gelu(x):
    return torch.nn.functional.gelu(x, approximate="tanh")
def _silu(x):
    return torch.nn.functional.silu(x)
def _sigmoid(x):
    return torch.sigmoid(x)
def _layernorm(x, axis=-1, eps=1e-5):
    m = x.mean(dim=axis, keepdim=True)
    v = x.var(dim=axis, keepdim=True, unbiased=False)
    return (x - m) / torch.sqrt(v + eps)
def _scatter_add(tgt, idx, src, axis=0):
    return tgt.clone().index_add_(axis, idx, src)
def _segment_sum(src, seg, n):
    out = torch.zeros((n,) + src.shape[1:], dtype=src.dtype)
    return out.index_add_(0, seg, src)
def _pad(x, pw):
    flat = tuple(v for pair in reversed(pw) for v in pair)
    return torch.nn.functional.pad(x, flat)

kernel_name = 'concat_heads__sliding_block__spectral_fnet__prompt'

def setup_inputs():

    _w = (lambda a: (lambda t: t.to(torch.bfloat16) if t.is_floating_point() else t)(torch.as_tensor(a)))
    def _A_setup():
        B, S, D = 1, 2048, 448
        return {
            "x": _w((lambda *s: torch.randn(*s, dtype=torch.bfloat16) * 0.2)(B, S, D)),
            "wqkv": _w((lambda *s: torch.randn(*s, dtype=torch.bfloat16) * 0.8)(D, 3 * D)),
            "wo": _w((lambda *s: torch.randn(*s, dtype=torch.bfloat16) * 0.2)(D, D)),
        }
    _A_in = _A_setup()

    def _B_setup():
        B, S, D = 1, 2048, 448
        ns = torch.arange(S)[:, None] * torch.arange(S)[None, :]
        nd = torch.arange(D)[:, None] * torch.arange(D)[None, :]
        Ws_re = torch.cos(-2.0 * torch.pi * ns / S).to(float)
        Ws_im = torch.sin(-2.0 * torch.pi * ns / S).to(float)
        Wd_re = torch.cos(-2.0 * torch.pi * nd / D).to(float)
        Wd_im = torch.sin(-2.0 * torch.pi * nd / D).to(float)
        return {
            "x": _w((lambda *s: torch.randn(*s, dtype=torch.bfloat16) * 0.05)(B, S, D)),
            "dft_seq_re": _w(Ws_re),
            "dft_seq_im": _w(Ws_im),
            "dft_hid_re": _w(Wd_re),
            "dft_hid_im": _w(Wd_im),
        }
    _B_in = _B_setup()

    return {
        "A_x": _A_in["x"],
        "A_wqkv": _A_in["wqkv"],
        "A_wo": _A_in["wo"],
        "B_x": _B_in["x"],
        "B_dft_seq_re": _B_in["dft_seq_re"],
        "B_dft_seq_im": _B_in["dft_seq_im"],
        "B_dft_hid_re": _B_in["dft_hid_re"],
        "B_dft_hid_im": _B_in["dft_hid_im"],
    }

def reference(A_x, A_wqkv, A_wo, B_x, B_dft_seq_re, B_dft_seq_im, B_dft_hid_re, B_dft_hid_im):
    def _A_ref(x, wqkv, wo):
        B, S, D = x.shape
        H = 16; Dh = D // H; W = 256
        qkv = (lambda x, *d: x.permute(*d))((x @ wqkv).reshape(B, S, 3, H, Dh), 2, 0, 3, 1, 4)
        q, k, v = qkv[0], qkv[1], qkv[2]
        scores = torch.einsum("bhqd,bhkd->bhqk", q, k) * (Dh ** -0.5)
        idx = torch.arange(S)
        mask = (idx[:, None] - idx[None, :] >= 0) & (idx[:, None] - idx[None, :] < W)
        scores = torch.where(mask[None, None], scores, -1e9)
        p = _softmax(scores, axis=-1)
        o = torch.einsum("bhqk,bhkd->bhqd", p, v)
        return (lambda x, *d: x.permute(*d))(o, 0, 2, 1, 3).reshape(B, S, D) @ wo

    def _B_ref(x, dft_seq_re, dft_seq_im, dft_hid_re, dft_hid_im):
        a = torch.einsum("bsd,ed->bse", x, dft_hid_re)
        b = torch.einsum("bsd,ed->bse", x, dft_hid_im)
        y = torch.einsum("ts,bse->bte", dft_seq_re, a) - torch.einsum("ts,bse->bte", dft_seq_im, b)
        return y

    a = _A_ref(A_x, A_wqkv, A_wo)
    b = _B_ref(B_x, B_dft_seq_re, B_dft_seq_im, B_dft_hid_re, B_dft_hid_im)
    return torch.concatenate([a, b], axis=-1)
```

```cpp
#include <hip/hip_runtime.h>
#include <hip/hip_bf16.h>
#include <stdint.h>

typedef unsigned short u16;
typedef unsigned long long u64;
typedef short bf16x8 __attribute__((ext_vector_type(8)));
typedef short s16x4 __attribute__((ext_vector_type(4)));
typedef float f32x4 __attribute__((ext_vector_type(4)));
typedef float f32x16 __attribute__((ext_vector_type(16)));
typedef unsigned u32x4 __attribute__((ext_vector_type(4)));
typedef unsigned u32x2 __attribute__((ext_vector_type(2)));
#define LDSAS __attribute__((address_space(3)))
#define GAS __attribute__((address_space(1)))
typedef LDSAS const char* lds_cptr;
typedef LDSAS char* lds_ptr;
typedef GAS u64 gu64;

constexpr int S = 2048, D = 448, NH = 16, DH = 28, NQKV = 1344;

#define MFMA16(a, b, c) __builtin_amdgcn_mfma_f32_16x16x32_bf16(a, b, c, 0, 0, 0)
#define MFMA32(a, b, c) __builtin_amdgcn_mfma_f32_32x32x16_bf16(a, b, c, 0, 0, 0)
#define SB() __builtin_amdgcn_sched_barrier(0)
#define FENCE() asm volatile("" ::: "memory")
#define PIN(x) asm volatile("" : "+v"(x))
#define LGKM0() do { FENCE(); __builtin_amdgcn_s_waitcnt(0xC07F); FENCE(); } while (0)
#define BAR() do { FENCE(); __builtin_amdgcn_s_waitcnt(0xC07F); __builtin_amdgcn_s_barrier(); FENCE(); } while (0)
#define VMW(n) asm volatile("s_waitcnt vmcnt(%0)" ::"i"(n) : "memory")
#define RLX_AGENT __ATOMIC_RELAXED, __HIP_MEMORY_SCOPE_AGENT
constexpr unsigned long long MASK56 = 0x00FFFFFFFFFFFFFFull;

template <int P>
__device__ __forceinline__ void dma4(const void* sbase, unsigned v0, unsigned v1, unsigned v2, unsigned v3, unsigned lds_dst) {
    asm volatile("s_mov_b32 m0, %5\n\ts_nop 0\n\tglobal_load_lds_dwordx4 %0, %4\n\tglobal_load_lds_dwordx4 %1, %4 offset:%6\n\t"
                 "global_load_lds_dwordx4 %2, %4 offset:%7\n\tglobal_load_lds_dwordx4 %3, %4 offset:%8"
                 ::"v"(v0), "v"(v1), "v"(v2), "v"(v3), "s"(sbase), "s"(lds_dst), "i"(P), "i"(2 * P), "i"(3 * P) : "memory");
}
template <int P>
__device__ __forceinline__ void dma4m(const void* sbase, unsigned v0, unsigned v1, unsigned v2, unsigned v3, unsigned lds_dst) {
    asm volatile("s_mov_b32 m0, %5\n\ts_mov_b64 exec, %9\n\tglobal_load_lds_dwordx4 %0, %4\n\tglobal_load_lds_dwordx4 %1, %4 offset:%6\n\t"
                 "global_load_lds_dwordx4 %2, %4 offset:%7\n\tglobal_load_lds_dwordx4 %3, %4 offset:%8\n\ts_mov_b64 exec, -1"
                 ::"v"(v0), "v"(v1), "v"(v2), "v"(v3), "s"(sbase), "s"(lds_dst), "i"(P), "i"(2 * P), "i"(3 * P), "s"(MASK56) : "memory");
}
template <int P>
__device__ __forceinline__ void dma3(const void* sbase, unsigned v0, unsigned v1, unsigned v2, unsigned lds_dst) {
    asm volatile("s_mov_b32 m0, %4\n\ts_nop 0\n\tglobal_load_lds_dwordx4 %0, %3\n\tglobal_load_lds_dwordx4 %1, %3 offset:%5\n\t"
                 "global_load_lds_dwordx4 %2, %3 offset:%6"
                 ::"v"(v0), "v"(v1), "v"(v2), "s"(sbase), "s"(lds_dst), "i"(P), "i"(2 * P) : "memory");
}
template <int P>
__device__ __forceinline__ void dma2m(const void* sbase, unsigned v0, unsigned v1, unsigned lds_dst) {
    asm volatile("s_mov_b32 m0, %3\n\ts_mov_b64 exec, %5\n\tglobal_load_lds_dwordx4 %0, %2\n\tglobal_load_lds_dwordx4 %1, %2 offset:%4\n\ts_mov_b64 exec, -1"
                 ::"v"(v0), "v"(v1), "s"(sbase), "s"(lds_dst), "i"(P), "s"(MASK56) : "memory");
}
__device__ __forceinline__ void dma1m(const void* sbase, unsigned v0, unsigned lds_dst) {
    asm volatile("s_mov_b32 m0, %2\n\ts_mov_b64 exec, %3\n\tglobal_load_lds_dwordx4 %0, %1\n\ts_mov_b64 exec, -1"
                 ::"v"(v0), "s"(sbase), "s"(lds_dst), "s"(MASK56) : "memory");
}
typedef __bf16 bf16x2_t __attribute__((ext_vector_type(2)));
typedef float f32x2 __attribute__((ext_vector_type(2)));
__device__ __forceinline__ unsigned cvtpk(float lo, float hi) {
    return __builtin_bit_cast(unsigned, __builtin_convertvector((f32x2){lo, hi}, bf16x2_t));
}
__device__ __forceinline__ s16x4 trread(lds_cptr p) {
    return __builtin_bit_cast(s16x4, __builtin_amdgcn_ds_read_tr16_b64_v4i16((LDSAS s16x4*)p));
}
__device__ __forceinline__ bf16x8 cat8(s16x4 a, s16x4 b) { return (bf16x8){a[0], a[1], a[2], a[3], b[0], b[1], b[2], b[3]}; }
__device__ __forceinline__ bf16x8 ldsread16(lds_cptr p) { return *(const LDSAS bf16x8*)p; }
__device__ __forceinline__ bf16x8 pack8(const f32x16& a, int b) {
    u32x4 w = {cvtpk(a[b], a[b + 1]), cvtpk(a[b + 2], a[b + 3]), cvtpk(a[b + 4], a[b + 5]), cvtpk(a[b + 6], a[b + 7])};
    return __builtin_bit_cast(bf16x8, w);
}
__device__ __forceinline__ int rfl(int x) { return __builtin_amdgcn_readfirstlane(x); }

#define STAMP(i) do {} while (0)
constexpr unsigned WS_PQ = 0;
constexpr unsigned WS_O = 128u * 28 * 1024;
constexpr unsigned WS_DATA_BYTES = WS_O + 128u * 16 * 1024;
constexpr size_t WS_SYNC = (size_t)8 << 20;
constexpr int SY_ACKB = 0, SY_ACKA = 32 * 16, SY_REQB = SY_ACKA + 8 * 16, SY_REQA = SY_REQB + 128 * 16;
constexpr size_t WS_TS = (size_t)64 << 20;
constexpr int LDS_TOTAL = 163840;
constexpr int LDS_SY = LDS_TOTAL - 256;
constexpr unsigned SPIN_CAP = 1u << 16;

struct Params {
    const u16* Ax; const u16* Wqkv; const u16* Wo; const u16* Bx; const u16* Cre; const u16* Cim; const u16* Wre; const u16* Wim;
    char* ws; u16* out;
};

__device__ __forceinline__ void sy_register(const Params& P, char* lds, int lane, int np, int ack_idx, int req_idx) {
    gu64* sy = (gu64*)(P.ws + WS_SYNC);
    if (lane < np) {
        const u64 a = __hip_atomic_load(sy + ack_idx + lane, RLX_AGENT);
        __hip_atomic_store(sy + req_idx + lane * 16, a + 1, RLX_AGENT);
        *(LDSAS u64*)((lds_ptr)lds + LDS_SY + lane * 8) = a + 1;
    }
}
__device__ __forceinline__ void sy_precheck(const Params& P, char* lds, int lane, int nc, int ack_idx, int req_row) {
    gu64* sy = (gu64*)(P.ws + WS_SYNC);
    const u64 want = __hip_atomic_load(sy + ack_idx, RLX_AGENT) + 1;
    for (unsigned spins = 0; spins < SPIN_CAP; ++spins) {
        bool ok = true;
        if (lane < nc) ok = __hip_atomic_load(sy + req_row + lane, RLX_AGENT) == want;
        if (__all(ok)) break;
        __builtin_amdgcn_s_sleep(2);
    }
    if (lane == 0) *(LDSAS u64*)((lds_ptr)lds + LDS_SY + 128) = want;
}
__device__ __forceinline__ void sy_publish(const Params& P, char* lds, int lane, int ack_idx) {
    gu64* sy = (gu64*)(P.ws + WS_SYNC);
    if (lane == 0) __hip_atomic_store(sy + ack_idx, *(const LDSAS u64*)((lds_cptr)lds + LDS_SY + 128), RLX_AGENT);
}
__device__ __forceinline__ void sy_wait(const Params& P, char* lds, int lane, int np, int ack_idx) {
    gu64* sy = (gu64*)(P.ws + WS_SYNC);
    const u64 want = *(const LDSAS u64*)((lds_cptr)lds + LDS_SY + (lane & 15) * 8);
    for (unsigned spins = 0; spins < SPIN_CAP; ++spins) {
        bool ok = true;
        if (lane < np) ok = __hip_atomic_load(sy + ack_idx + lane, RLX_AGENT) == want;
        if (__all(ok)) break;
        __builtin_amdgcn_s_sleep(1);
    }
    FENCE();
}
__device__ __forceinline__ __amdgpu_buffer_rsrc_t ws_rsrc(const Params& P) {
    return __builtin_amdgcn_make_buffer_rsrc((void*)P.ws, 0, WS_DATA_BYTES, 0x00020000);
}
__device__ __forceinline__ void st_sc1(__amdgpu_buffer_rsrc_t rs, unsigned off, bf16x8 v) {
    __builtin_amdgcn_raw_buffer_store_b128(__builtin_bit_cast(u32x4, v), rs, off, 0, 16);
}
__device__ __forceinline__ bf16x8 ld_sc1(__amdgpu_buffer_rsrc_t rs, unsigned off) {
    return __builtin_bit_cast(bf16x8, __builtin_amdgcn_raw_buffer_load_b128(rs, off, 0, 16));
}

constexpr int T_STG = 143360;

template <int NE>
struct TailB {
    static constexpr int SL = 2 * NE * 16 * 224;
    unsigned v[4]; const char* wb; unsigned wd;
    __device__ __forceinline__ void init(const Params& P, char* lds, int lane, int lw, int e0) {
        const int l = lane < 56 ? lane : 55;
#pragma unroll
        for (int i = 0; i < 4; ++i) v[i] = (4 * i + l / 14) * (D * 2) + (l % 14) * 16 + (3 - i) * 896;
        wb = (const char*)(((lw >> 1) ? P.Wim : P.Wre) + (size_t)(e0 + (lw & 1) * (NE * 8)) * D) - 3 * 896;
        wd = (unsigned)(uintptr_t)lds + lw * (NE * 2) * 896;
    }
    __device__ __forceinline__ void issue(int j) {
        const char* p = wb + j * 224; const unsigned d = wd + j * SL;
        dma4m<896>(p, v[0], v[1], v[2], v[3], d);
        dma4m<896>(p + 16 * D * 2, v[0], v[1], v[2], v[3], d + 4 * 896);
        if (NE == 5) dma2m<896>(p + 32 * D * 2, v[0], v[1], d + 8 * 896);
    }
};
template <int NE>
__device__ __forceinline__ void tail_b_compute(const Params& P, char* lds, int lane, int wid, int tb, int e0) {
    constexpr int SL = TailB<NE>::SL;
    const int fr = lane & 15, g = lane >> 4;
    const __amdgpu_buffer_rsrc_t rs = ws_rsrc(P);
    const unsigned pq = WS_PQ + (unsigned)(tb * 4 + wid) * 28 * 1024 + lane * 16;
    bf16x8 af[28];
#pragma unroll
    for (int s = 0; s < 28; ++s) af[s] = ld_sc1(rs, pq + s * 1024);
    f32x4 acc[NE];
#pragma unroll
    for (int n = 0; n < NE; ++n) acc[n] = (f32x4){0, 0, 0, 0};
    lds_cptr wr = (lds_cptr)lds + fr * 224 + g * 16;
    lds_cptr wm = (lds_cptr)lds + (g >> 1) * (NE * 16 * 224) + fr * 224 + 192 + (g & 1) * 16;
    bf16x8 wf[NE], wn[NE];
#pragma unroll
    for (int n = 0; n < NE; ++n) wf[n] = ldsread16(wr + n * 16 * 224);
#pragma unroll
    for (int u = 0; u < 28; ++u) {
        const int u1 = u + 1, j1 = u1 / 7, s1 = u1 % 7;
#pragma unroll
        for (int n = 0; n < NE; ++n) {
            acc[n] = MFMA16(wf[n], af[u], acc[n]); PIN(acc[n]); SB();
            if (u1 < 28) {
                wn[n] = (s1 < 3) ? ldsread16(wr + j1 * SL + n * 16 * 224 + s1 * 64)
                      : (s1 < 6) ? ldsread16(wr + j1 * SL + NE * 16 * 224 + n * 16 * 224 + (s1 - 3) * 64)
                                 : ldsread16(wm + j1 * SL + n * 16 * 224);
                SB();
            }
        }
#pragma unroll
        for (int n = 0; n < NE; ++n) wf[n] = wn[n];
    }
    STAMP(12);
    constexpr int PIT = NE * 32 + 16;
    lds_ptr stg = (lds_ptr)lds + T_STG + wid * 4096;
#pragma unroll
    for (int n = 0; n < NE; ++n) {
        u32x2 w = {cvtpk(acc[n][0], acc[n][1]), cvtpk(acc[n][2], acc[n][3])};
        *(LDSAS u32x2*)(stg + fr * PIT + n * 32 + g * 8) = w;
    }
    LGKM0();
    u16* ob = P.out + (size_t)(tb * 64 + wid * 16) * 896 + 448 + e0;
#pragma unroll
    for (int i = 0; i < (16 * NE * 2 + 63) / 64; ++i) {
        const int e = i * 64 + lane;
        if (e < 16 * NE * 2) {
            const int row = e / (NE * 2), c = e % (NE * 2);
            *(u32x4*)((char*)(ob + (size_t)row * 896) + c * 16) = *(const LDSAS u32x4*)(stg + row * PIT + c * 16);
        }
    }
}

struct TailA {
    unsigned v[4]; const char* wb; unsigned wd;
    __device__ __forceinline__ void init(const Params& P, char* lds, int lane, int lw, int n0) {
        const int l = lane < 56 ? lane : 55;
#pragma unroll
        for (int i = 0; i < 4; ++i) v[i] = (4 * i + l / 14) * (D * 2) + (l % 14) * 16 + (3 - i) * 896;
        wb = (const char*)(P.Wo + (size_t)(lw * 28) * D + n0) - 3 * 896;
        wd = (unsigned)(uintptr_t)lds + lw * 7 * 896;
    }
    __device__ __forceinline__ void issue(int j) {
        const char* p = wb + (size_t)j * (112 * D * 2); const unsigned d = wd + j * (112 * 224);
        dma4m<896>(p, v[0], v[1], v[2], v[3], d);
        dma2m<896>(p + 16 * D * 2, v[0], v[1], d + 4 * 896);
        dma1m(p + 12 * D * 2 + 3 * 896, v[3], d + 6 * 896);
    }
};
__device__ __forceinline__ void tail_a_compute(const Params& P, char* lds, int lane, int wid, int tq, int n0) {
    const int fr = lane & 15, g = lane >> 4;
    const __amdgpu_buffer_rsrc_t rs = ws_rsrc(P);
    const unsigned of = WS_O + (unsigned)(tq * 8 + wid * 2) * 16 * 1024 + lane * 16;
    bf16x8 a0[16], a1[16];
#pragma unroll
    for (int s = 0; s < 16; ++s) { a0[s] = ld_sc1(rs, of + s * 1024); a1[s] = ld_sc1(rs, of + 16384 + s * 1024); }
    f32x4 acc0[7], acc1[7];
#pragma unroll
    for (int n = 0; n < 7; ++n) { acc0[n] = (f32x4){0, 0, 0, 0}; acc1[n] = (f32x4){0, 0, 0, 0}; }
    const int q = fr >> 2, p4 = lane & 3;
    const int d0 = (g == 0) ? 0 : (g == 1) ? 4 : (g == 2) ? 16 : 20;
    const int d1 = (g == 0) ? 8 : (g == 1) ? 12 : 24;
    lds_cptr r0 = (lds_cptr)lds + (d0 + q) * 224 + p4 * 8;
    lds_cptr r1 = (lds_cptr)lds + (d1 + q) * 224 + p4 * 8;
    s16x4 lo[7], hi[7], ln[7], hn[7];
#pragma unroll
    for (int n = 0; n < 7; ++n) { lo[n] = trread(r0 + n * 32); hi[n] = trread(r1 + n * 32); }
#pragma unroll
    for (int h = 0; h < 16; ++h) {
#pragma unroll
        for (int n = 0; n < 7; ++n) {
            const bf16x8 wf = cat8(lo[n], hi[n]);
            acc0[n] = MFMA16(wf, a0[h], acc0[n]); PIN(acc0[n]); SB();
            if (h < 15) { ln[n] = trread(r0 + (h + 1) * (28 * 224) + n * 32); SB(); }
            acc1[n] = MFMA16(wf, a1[h], acc1[n]); PIN(acc1[n]); SB();
            if (h < 15) { hn[n] = trread(r1 + (h + 1) * (28 * 224) + n * 32); SB(); }
        }
        if (h < 15) {
#pragma unroll
            for (int n = 0; n < 7; ++n) { lo[n] = ln[n]; hi[n] = hn[n]; }
        }
    }
    STAMP(12);
    constexpr int PIT = 240;
    lds_ptr stg = (lds_ptr)lds + 100352 + wid * 8192;
#pragma unroll
    for (int n = 0; n < 7; ++n) {
        u32x2 w0 = {cvtpk(acc0[n][0], acc0[n][1]), cvtpk(acc0[n][2], acc0[n][3])};
        u32x2 w1 = {cvtpk(acc1[n][0], acc1[n][1]), cvtpk(acc1[n][2], acc1[n][3])};
        *(LDSAS u32x2*)(stg + fr * PIT + n * 32 + g * 8) = w0;
        *(LDSAS u32x2*)(stg + (16 + fr) * PIT + n * 32 + g * 8) = w1;
    }
    LGKM0();
    u16* ob = P.out + (size_t)(tq * 128 + wid * 32) * 896 + n0;
#pragma unroll
    for (int i = 0; i < 7; ++i) {
        const int e = i * 64 + lane;
        const int row = e / 14, c = e % 14;
        *(u32x4*)((char*)(ob + (size_t)row * 896) + c * 16) = *(const LDSAS u32x4*)(stg + row * PIT + c * 16);
    }
}

constexpr int B_CT = 16384, B_XP = 960, B_XT = 16 * B_XP, B_STAGE = B_CT + B_XT;
constexpr int B_NS = 5;
constexpr int B_RED = 73728, B_IMG = 131072;
#define SBS 8

__device__ __forceinline__ void role_b(const Params& P, char* lds, int kx, int ii, int dj) {
    STAMP(0);
    const int tid = threadIdx.x, lane = tid & 63;
    const int wid = rfl(tid >> 6);
    const unsigned lds0 = (unsigned)(uintptr_t)lds;
    const int tb = 4 * kx + ii, t0 = tb * 64, rot = (ii + dj) & 3;
    const int e0 = 80 * dj;
    if (wid >= 4) {
        const int lw = wid - 4;
        unsigned cv[4], xv[4];
        {
            const int r = lane >> 3, c = lane & 7, l = lane < 56 ? lane : 55;
#pragma unroll
            for (int i = 0; i < 4; ++i) {
                cv[i] = (8 * i + r) * (S * 2) + ((c ^ ((4 * i + (r >> 1)) & 7)) * 16) + (3 - i) * 1024;
                xv[i] = (4 * i + l / 14) * (D * 2) + (l % 14) * 16 + (3 - i) * B_XP;
            }
        }
        const char* cb0 = (const char*)((lw < 2 ? P.Cre : P.Cim) + (size_t)(t0 + (lw & 1) * 32) * S) - 3 * 1024;
        const char* xb0 = (const char*)(P.Bx + (size_t)(lw * 16) * D + dj * 112) - 3 * B_XP;
        const unsigned cd = lds0 + lw * 4096, xd = lds0 + B_CT + lw * 4 * B_XP;
        auto issue = [&](int s, int slot) {
            const int ks = (s & ~(SBS - 1)) | ((s + rot * (SBS / 4)) & (SBS - 1));
            const unsigned so = slot * B_STAGE;
            dma4<1024>(cb0 + ks * 128, cv[0], cv[1], cv[2], cv[3], cd + so);
            dma4m<B_XP>(xb0 + (size_t)ks * (64 * D * 2), xv[0], xv[1], xv[2], xv[3], xd + so);
        };
        issue(0, 0); issue(1, 1);
        VMW(8); BAR();
        issue(2, 2); issue(3, 3);
        int sl = 4;
        for (int p = 0; p < 16; ++p) {
            if (p < 15) VMW(8); else VMW(0);
            BAR();
            if (p < 14) {
                issue(2 * p + 4, sl); sl = (sl == B_NS - 1) ? 0 : sl + 1;
                issue(2 * p + 5, sl); sl = (sl == B_NS - 1) ? 0 : sl + 1;
            }
        }
        if (wid == 4) sy_precheck(P, lds, lane, 6, SY_ACKB + tb * 16 + dj, SY_REQB + (tb * 4 + dj) * 16);
        TailB<5> tw; tw.init(P, lds, lane, lw, e0);
        BAR();
        tw.issue(0); tw.issue(1);
        BAR();
        BAR();
        tw.issue(2); tw.issue(3);
        VMW(0);
        BAR();
        return;
    }
    if (wid == 0) sy_register(P, lds, lane, 4, SY_ACKB + tb * 16, SY_REQB + (tb * 4) * 16 + dj);
    const int tw = wid & 1, kh = wid >> 1;
    const int fr = lane & 15, g = lane >> 4;
    const int crow = 32 * tw + fr;
    const int coff = crow * 128 + (((kh * 4 + g) ^ ((crow >> 1) & 7)) * 16);
    const int q = fr >> 2, p4 = lane & 3;
    const int xr0 = kh * 32 + 8 * g + q;
    const int xoff0 = B_CT + (xr0 >> 2) * B_XP + (xr0 & 3) * 224 + p4 * 8;
    f32x4 acc[4][7];
#pragma unroll
    for (int m = 0; m < 4; ++m)
#pragma unroll
        for (int n = 0; n < 7; ++n) acc[m][n] = (f32x4){0, 0, 0, 0};
    bf16x8 cfA[4], xfA[7], cfB[4], xfB[7];
#define MM(cf, xf, m, n) do { acc[m][n] = MFMA16(xf[n], cf[m], acc[m][n]); PIN(acc[m][n]); } while (0)
#define RL(t, st_, n) t = trread(st_ + xoff0 + (n) * 32)
#define RH(t, st_, n) t = trread(st_ + xoff0 + B_XP + (n) * 32)
#define RC(d, st_, m) d = ldsread16(st_ + coff + ((m) & 1) * 2048 + ((m) >> 1) * 8192)
#define G4(cf, xf, n, R0, R1, R2) do { MM(cf, xf, 0, n); SB(); R0; SB(); MM(cf, xf, 1, n); SB(); R1; SB(); MM(cf, xf, 2, n); SB(); R2; SB(); MM(cf, xf, 3, n); SB(); } while (0)
#define NOP_ do {} while (0)
#define B_STEP(cf, xf, cn, xn, st_) do { s16x4 lo[7], hi[7]; \
        G4(cf, xf, 0, RC(cn[0], st_, 0), RC(cn[1], st_, 1), RC(cn[2], st_, 2)); \
        G4(cf, xf, 1, RC(cn[3], st_, 3), RL(lo[0], st_, 0), RH(hi[0], st_, 0)); \
        G4(cf, xf, 2, RL(lo[1], st_, 1), RH(hi[1], st_, 1), RL(lo[2], st_, 2)); \
        G4(cf, xf, 3, RH(hi[2], st_, 2), RL(lo[3], st_, 3), RH(hi[3], st_, 3)); \
        G4(cf, xf, 4, RL(lo[4], st_, 4), RH(hi[4], st_, 4), RL(lo[5], st_, 5)); \
        G4(cf, xf, 5, RH(hi[5], st_, 5), RL(lo[6], st_, 6), RH(hi[6], st_, 6)); \
        G4(cf, xf, 6, NOP_, NOP_, NOP_); \
        _Pragma("unroll") for (int n_ = 0; n_ < 7; ++n_) xn[n_] = cat8(lo[n_], hi[n_]); } while (0)
    BAR();
    STAMP(1);
    {
        lds_cptr st_ = (lds_cptr)lds; s16x4 lo[7], hi[7];
        RC(cfA[0], st_, 0); RC(cfA[1], st_, 1); RC(cfA[2], st_, 2); RC(cfA[3], st_, 3);
#pragma unroll
        for (int n = 0; n < 7; ++n) { RL(lo[n], st_, n); RH(hi[n], st_, n); xfA[n] = cat8(lo[n], hi[n]); }
    }
    int sl = 1;
    for (int p = 0; p < 15; ++p) {
        if (p == 4) STAMP(2);
        if (p == 8) STAMP(3);
        BAR(); SB();
        { lds_cptr st_ = (lds_cptr)lds + sl * B_STAGE; B_STEP(cfA, xfA, cfB, xfB, st_); sl = (sl == B_NS - 1) ? 0 : sl + 1; }
        { lds_cptr st_ = (lds_cptr)lds + sl * B_STAGE; B_STEP(cfB, xfB, cfA, xfA, st_); sl = (sl == B_NS - 1) ? 0 : sl + 1; }
    }
    BAR(); SB();
    { lds_cptr st_ = (lds_cptr)lds + sl * B_STAGE; B_STEP(cfA, xfA, cfB, xfB, st_); }
#pragma unroll
    for (int n = 0; n < 7; ++n) { MM(cfB, xfB, 0, n); MM(cfB, xfB, 1, n); MM(cfB, xfB, 2, n); MM(cfB, xfB, 3, n); }
    BAR();
    STAMP(4);
    LDSAS f32x4* red = (LDSAS f32x4*)((lds_ptr)lds + B_RED) + lane;
    if (kh == 0) {
#pragma unroll
        for (int n = 0; n < 7; ++n) { red[((tw * 2 + 0) * 14 + n) * 64] = acc[1][n]; red[((tw * 2 + 0) * 14 + 7 + n) * 64] = acc[3][n]; }
    } else {
#pragma unroll
        for (int n = 0; n < 7; ++n) { red[((tw * 2 + 1) * 14 + n) * 64] = acc[0][n]; red[((tw * 2 + 1) * 14 + 7 + n) * 64] = acc[2][n]; }
    }
    BAR();
    lds_ptr img = (lds_ptr)lds + B_IMG + wid * 8192;
#define FIN(MP_, MQ_) do { const LDSAS f32x4* rd = red + (tw * 2 + (1 - kh)) * 14 * 64; \
        _Pragma("unroll") for (int n = 0; n < 7; ++n) { \
            const f32x4 a = acc[MP_][n] + rd[n * 64], b = acc[MQ_][n] + rd[(7 + n) * 64]; \
            u32x2 wp = {cvtpk(a[0], a[1]), cvtpk(a[2], a[3])}, wq = {cvtpk(-b[0], -b[1]), cvtpk(-b[2], -b[3])}; \
            *(LDSAS u32x2*)(img + fr * 240 + (16 * n + 4 * g) * 2) = wp; \
            *(LDSAS u32x2*)(img + 3840 + fr * 240 + (16 * n + 4 * g) * 2) = wq; } } while (0)
    if (kh == 0) FIN(0, 2); else FIN(1, 3);
#undef FIN
    LGKM0();
    {
        const __amdgpu_buffer_rsrc_t rs = ws_rsrc(P);
        const unsigned dst = WS_PQ + ((unsigned)(tb * 4 + tw * 2 + kh) * 28 + dj * 7) * 1024 + lane * 16;
        lds_cptr rp = (lds_cptr)img + fr * 240 + g * 16;
#pragma unroll
        for (int i = 0; i < 3; ++i) {
            st_sc1(rs, dst + i * 1024, ldsread16(rp + i * 64));
            st_sc1(rs, dst + (3 + i) * 1024, ldsread16(rp + 3840 + i * 64));
        }
        st_sc1(rs, dst + 6 * 1024, ldsread16((lds_cptr)img + (g >> 1) * 3840 + fr * 240 + 192 + (g & 1) * 16));
    }
    STAMP(5);
    VMW(0);
    BAR();
    if (wid == 0) { sy_publish(P, lds, lane, SY_ACKB + tb * 16 + dj); STAMP(10); sy_wait(P, lds, lane, 4, SY_ACKB + tb * 16); }
    BAR();
    STAMP(11);
    tail_b_compute<5>(P, lds, lane, wid, tb, e0);
    STAMP(13);
#undef MM
#undef RL
#undef RH
#undef RC
#undef G4
#undef B_STEP
}

constexpr int A_XS = 32768;
constexpr int A_WOFF = 4 * A_XS;
constexpr int A_WS = 12288;
constexpr int A_KV = 0, A_Q = 65536;

__device__ __forceinline__ void role_a(const Params& P, char* lds, int qb, int h) {
    STAMP(0);
    const int tid = threadIdx.x, lane = tid & 63;
    const int wid = rfl(tid >> 6);
    const unsigned lds0 = (unsigned)(uintptr_t)lds;
    const int q0 = qb * 256;
    const int rot = h % 7;
    const int r32 = lane & 31, hi = lane >> 5;
    const bool tailA = h < 8;
    const int tq = 2 * qb + (h >> 2), n0 = 112 * (h & 3);
    const int tb = 4 * qb + ((h - 8) >> 1), eg = 4 + (h & 1), e0 = 320 + 64 * (h & 1);
    if (wid >= 4) {
        const int lw = wid - 4;
        unsigned xv[4], wv[3];
        {
            const int r = lane >> 3, c = lane & 7;
#pragma unroll
            for (int i = 0; i < 4; ++i) xv[i] = (8 * i + r) * (D * 2) + ((c ^ ((4 * i + (r >> 1)) & 7)) * 16) + (3 - i) * 1024;
            const int c4 = lane & 3, kr = lane >> 2;
#pragma unroll
            for (int sec = 0; sec < 3; ++sec) wv[sec] = (kr * NQKV + sec * D + (c4 < 3 ? 8 * c4 : 20)) * 2 + (2 - sec) * 1024;
        }
        const int rown = q0 + 64 * lw, rprev = (qb > 0) ? rown - 256 : rown;
        const char* xbp = (const char*)(P.Ax + (size_t)rprev * D) - 3 * 1024;
        const char* xbo = (const char*)(P.Ax + (size_t)rown * D) - 3 * 1024;
        const char* wb = (const char*)(P.Wqkv + (size_t)(16 * lw) * NQKV + h * DH) - 2 * 1024;
        auto kcof = [&](int c) { int k = c + rot; return k >= 7 ? k - 7 : k; };
        auto issue_x = [&](int j) {
            const int off = kcof(j >> 1) * 128 + (j & 1) * (32 * D * 2);
            const unsigned dst = lds0 + (j & 3) * A_XS + lw * 8192;
            dma4<1024>(xbp + off, xv[0], xv[1], xv[2], xv[3], dst);
            dma4<1024>(xbo + off, xv[0], xv[1], xv[2], xv[3], dst + 4096);
        };
        auto issue_w = [&](int c) {
            dma3<1024>(wb + (size_t)kcof(c) * (64 * NQKV * 2), wv[0], wv[1], wv[2], lds0 + A_WOFF + (c & 1) * A_WS + lw * 3072);
        };
        issue_w(0); issue_x(0); issue_x(1);
        VMW(0);
        BAR();
        issue_w(1); issue_x(2); issue_x(3);
        for (int j = 1; j < 11; j += 2) {
            VMW(8); BAR();
            issue_x(j + 3); issue_w((j + 3) >> 1);
            VMW(11); BAR();
            issue_x(j + 4);
        }
        VMW(8); BAR();
        VMW(0); BAR();
        BAR();
        BAR();
        if (wid == 4) sy_precheck(P, lds, lane, 8, SY_ACKA + qb * 16 + h, SY_REQA + (qb * 16 + h) * 16);
    } else {
        if (wid == 0) {
            if (tailA) sy_register(P, lds, lane, 16, SY_ACKA + qb * 16, SY_REQA + (qb * 16) * 16 + (h & 7));
            else sy_register(P, lds, lane, 4, SY_ACKB + tb * 16, SY_REQB + (tb * 4) * 16 + eg);
        }
        lds_cptr xb = (lds_cptr)lds + wid * 8192 + r32 * 128;
        const int xf = (r32 >> 1) & 7;
        int xo4[4];
#pragma unroll
        for (int ks = 0; ks < 4; ++ks) xo4[ks] = ((2 * ks + hi) ^ xf) * 16;
        const int wq_ = (lane & 15) >> 2, wp = lane & 3, wc = (lane >> 4) & 1;
        lds_cptr wb = (lds_cptr)lds + A_WOFF + (8 * hi + wq_) * 64 + wc * 32 + wp * 8;
        f32x16 aQ0 = {}, aKo0 = {}, aVo0 = {}, aKp0 = {}, aVp0 = {}, aQ1 = {}, aKo1 = {}, aVo1 = {}, aKp1 = {}, aVp1 = {};
        bf16x8 fq[4], fk[4], fv[4], xpA, xoA, xpB, xoB;
        s16x4 wl, wh;
#define RXP(d, j, ks) d = ldsread16(xb + ((j) & 3) * A_XS + xo4[ks])
#define RXO(d, j, ks) d = ldsread16(xb + ((j) & 3) * A_XS + 4096 + xo4[ks])
#define RW(d, c, ks, sec) do { lds_cptr w_ = wb + ((c) & 1) * A_WS + (ks) * 3072 + (sec) * 1024; wl = trread(w_); wh = trread(w_ + 256); d = cat8(wl, wh); } while (0)
#define M32(acc, a, b) do { acc = MFMA32(a, b, acc); PIN(acc); SB(); } while (0)
#define A_KS(H, ks, xp, xo, xpn, xon, jn, kn, LX, LW, cn, wn) do { \
            M32(aQ##H, fq[ks], xo); if (LX) { RXP(xpn, jn, kn); } if (LW) { RW(fq[wn], cn, wn, 0); } SB(); \
            M32(aKo##H, fk[ks], xo); if (LX) { RXO(xon, jn, kn); } SB(); \
            M32(aVo##H, xo, fv[ks]); if (LW) { RW(fk[wn], cn, wn, 1); } SB(); \
            M32(aKp##H, fk[ks], xp); if (LW) { RW(fv[wn], cn, wn, 2); } SB(); \
            M32(aVp##H, xp, fv[ks]); } while (0)
        BAR();
        STAMP(1);
        RXP(xpA, 0, 0); RXO(xoA, 0, 0); RW(fq[0], 0, 0, 0); RW(fk[0], 0, 0, 1); RW(fv[0], 0, 0, 2);
        for (int c = 0; c < 7; ++c) {
            const int j = 2 * c;
            if (c == 1) STAMP(2);
            if (c == 4) STAMP(3);
            A_KS(0, 0, xpA, xoA, xpB, xoB, j, 1, true, true, c, 1);
            A_KS(0, 1, xpB, xoB, xpA, xoA, j, 2, true, true, c, 2);
            A_KS(0, 2, xpA, xoA, xpB, xoB, j, 3, true, true, c, 3);
            A_KS(0, 3, xpB, xoB, xpA, xoA, j + 1, 0, true, false, c, 0);
            BAR();
            A_KS(1, 0, xpA, xoA, xpB, xoB, j + 1, 1, true, false, c, 0);
            A_KS(1, 1, xpB, xoB, xpA, xoA, j + 1, 2, true, false, c, 0);
            A_KS(1, 2, xpA, xoA, xpB, xoB, j + 1, 3, true, false, c, 0);
            if (c < 6) {
                A_KS(1, 3, xpB, xoB, xpA, xoA, j + 2, 0, true, true, c + 1, 0);
                BAR();
            } else {
                A_KS(1, 3, xpB, xoB, xpA, xoA, j + 2, 0, false, false, c + 1, 0);
            }
        }
        STAMP(4);
        BAR();
        if (hi == 0) {
            aQ0[12] = 0.f; aQ0[13] = 0.f; aQ0[14] = 0.f; aQ0[15] = 0.f;
            aQ1[12] = 0.f; aQ1[13] = 0.f; aQ1[14] = 0.f; aQ1[15] = 0.f;
        }
        const bf16x8 ones = {0x3F80, 0x3F80, 0x3F80, 0x3F80, 0x3F80, 0x3F80, 0x3F80, 0x3F80};
        const bool oc = (r32 == 24);
        LDSAS bf16x8* kv = (LDSAS bf16x8*)((lds_ptr)lds + A_KV + lane * 16);
        LDSAS bf16x8* qd = (LDSAS bf16x8*)((lds_ptr)lds + A_Q + lane * 16);
#define PUTKV(tile, K, V) do { LDSAS bf16x8* d_ = kv + (tile) * 256; d_[0] = pack8(K, 0); d_[64] = pack8(K, 8); \
            d_[128] = oc ? ones : pack8(V, 0); d_[192] = oc ? ones : pack8(V, 8); } while (0)
        PUTKV(2 * wid, aKp0, aVp0); PUTKV(2 * wid + 1, aKp1, aVp1);
        PUTKV(8 + 2 * wid, aKo0, aVo0); PUTKV(8 + 2 * wid + 1, aKo1, aVo1);
        qd[(2 * wid) * 128] = pack8(aQ0, 0); qd[(2 * wid) * 128 + 64] = pack8(aQ0, 8);
        qd[(2 * wid + 1) * 128] = pack8(aQ1, 0); qd[(2 * wid + 1) * 128 + 64] = pack8(aQ1, 8);
#undef PUTKV
#undef RXP
#undef RXO
#undef RW
#undef M32
#undef A_KS
    }
    BAR();
    STAMP(5);
    lds_cptr kvb = (lds_cptr)lds + A_KV + wid * 4096 + lane * 16;
    const bf16x8 qf0 = ldsread16((lds_cptr)lds + A_Q + wid * 2048 + lane * 16), qf1 = ldsread16((lds_cptr)lds + A_Q + wid * 2048 + 1024 + lane * 16);
    f32x16 sc[9];
    const float NEG = -__builtin_inff();
    {
        bf16x8 ka = ldsread16(kvb), kb = ldsread16(kvb + 1024);
#pragma unroll
        for (int j = 0; j < 9; ++j) {
            bf16x8 na, nb;
            if (j < 8) { na = ldsread16(kvb + (j + 1) * 4096); nb = ldsread16(kvb + (j + 1) * 4096 + 1024); }
            SB();
            f32x16 z = {};
            z = MFMA32(ka, qf0, z);
            sc[j] = MFMA32(kb, qf1, z);
            PIN(sc[j]);
            SB();
            if (j < 8) { ka = na; kb = nb; }
        }
    }
    if (qb == 0) {
#pragma unroll
        for (int j = 0; j < 8; ++j)
            if (j < 8 - wid) {
#pragma unroll
                for (int r = 0; r < 16; ++r) sc[j][r] = NEG;
            }
    }
#pragma unroll
    for (int r = 0; r < 16; ++r) {
        const int kk = (r & 3) + 8 * (r >> 2) + 4 * hi;
        if (kk <= r32) sc[0][r] = NEG;
        if (kk > r32) sc[8][r] = NEG;
    }
    float m0 = sc[8][0], m1 = sc[8][1];
#pragma unroll
    for (int j = 0; j < 9; ++j)
#pragma unroll
        for (int r = 0; r < 16; r += 4) {
            m0 = fmaxf(fmaxf(m0, sc[j][r]), sc[j][r + 1]);
            m1 = fmaxf(fmaxf(m1, sc[j][r + 2]), sc[j][r + 3]);
        }
    float m = fmaxf(m0, m1);
    {
        auto rr = __builtin_amdgcn_permlane32_swap(__float_as_uint(m), __float_as_uint(m), false, false);
        m = fmaxf(__uint_as_float(rr[0]), __uint_as_float(rr[1]));
    }
    STAMP(6);
    constexpr float C2 = 0.18898223650461362f * 1.4426950408889634f;
    const float mc = -m * C2;
    f32x16 o = {};
#pragma unroll
    for (int j = 0; j < 9; ++j) {
        const bf16x8 v0 = ldsread16(kvb + j * 4096 + 2048), v1 = ldsread16(kvb + j * 4096 + 3072);
#pragma unroll
        for (int r = 0; r < 16; ++r) sc[j][r] = __builtin_amdgcn_exp2f(fmaf(sc[j][r], C2, mc));
        const bf16x8 p0 = pack8(sc[j], 0), p1 = pack8(sc[j], 8);
        o = MFMA32(v0, p0, o);
        o = MFMA32(v1, p1, o);
        PIN(o);
        SB();
    }
    float l = o[12];
    {
        auto rr = __builtin_amdgcn_permlane32_swap(__float_as_uint(l), __float_as_uint(l), false, false);
        l = __uint_as_float(rr[0]);
    }
    const float inv = 1.0f / l;
#pragma unroll
    for (int r = 0; r < 16; ++r) o[r] *= inv;
    if (hi == 0) { o[12] = 0.f; o[13] = 0.f; o[14] = 0.f; o[15] = 0.f; }
    {
        const __amdgpu_buffer_rsrc_t rs = ws_rsrc(P);
        const int t16 = (q0 + 32 * wid) / 16 + (r32 >> 4);
        const unsigned dst = WS_O + ((unsigned)t16 * 16 + h) * 1024 + hi * 256 + (r32 & 15) * 16;
        st_sc1(rs, dst, pack8(o, 0));
        st_sc1(rs, dst + 512, pack8(o, 8));
    }
    STAMP(7);
    VMW(0);
    BAR();
    if (wid >= 4) {
        const int lw = wid - 4;
        if (tailA) { TailA tw; tw.init(P, lds, lane, lw, n0); tw.issue(0); tw.issue(1); tw.issue(2); tw.issue(3); }
        else { TailB<4> tw; tw.init(P, lds, lane, lw, e0); tw.issue(0); tw.issue(1); tw.issue(2); tw.issue(3); }
        VMW(0);
        BAR();
        return;
    }
    if (wid == 0) {
        sy_publish(P, lds, lane, SY_ACKA + qb * 16 + h);
        STAMP(10);
        if (tailA) sy_wait(P, lds, lane, 16, SY_ACKA + qb * 16); else sy_wait(P, lds, lane, 4, SY_ACKB + tb * 16);
    }
    BAR();
    STAMP(11);
    if (tailA) tail_a_compute(P, lds, lane, wid, tq, n0); else tail_b_compute<4>(P, lds, lane, wid, tb, e0);
    STAMP(13);
}

__global__ __launch_bounds__(512, 2) void fused_kernel(Params P) {
    extern __shared__ __attribute__((aligned(16))) char lds[];
    const int b = blockIdx.x, k = b & 7, idx = b >> 3;
    if (idx < 16) role_b(P, lds, k, idx >> 2, idx & 3);
    else role_a(P, lds, k, idx - 16);
}

extern "C" void kernel_launch(void* const* d_in, const int* in_sizes, int n_in, void* d_out, int out_size, void* d_ws,
                              size_t ws_size, hipStream_t stream) {
    Params P;
    P.Ax = (const u16*)d_in[0]; P.Wqkv = (const u16*)d_in[1]; P.Wo = (const u16*)d_in[2]; P.Bx = (const u16*)d_in[3];
    P.Cre = (const u16*)d_in[4]; P.Cim = (const u16*)d_in[5]; P.Wre = (const u16*)d_in[6]; P.Wim = (const u16*)d_in[7];
    P.ws = (char*)d_ws; P.out = (u16*)d_out;
    (void)hipFuncSetAttribute((const void*)fused_kernel, hipFuncAttributeMaxDynamicSharedMemorySize, LDS_TOTAL);
    fused_kernel<<<256, 512, LDS_TOTAL, stream>>>(P);
}
```

```cpp
#include <hip/hip_runtime.h>
#include <hip/hip_bf16.h>
#include <stdint.h>

typedef unsigned short u16;
typedef unsigned long long u64;
typedef short bf16x8 __attribute__((ext_vector_type(8)));
typedef short s16x4 __attribute__((ext_vector_type(4)));
typedef float f32x4 __attribute__((ext_vector_type(4)));
typedef float f32x16 __attribute__((ext_vector_type(16)));
typedef unsigned u32x4 __attribute__((ext_vector_type(4)));
typedef unsigned u32x2 __attribute__((ext_vector_type(2)));
#define LDSAS __attribute__((address_space(3)))
#define GAS __attribute__((address_space(1)))
typedef LDSAS const char* lds_cptr;
typedef LDSAS char* lds_ptr;
typedef GAS u64 gu64;

constexpr int S = 2048, D = 448, NH = 16, DH = 28, NQKV = 1344;

#define MFMA16(a, b, c) __builtin_amdgcn_mfma_f32_16x16x32_bf16(a, b, c, 0, 0, 0)
#define MFMA32(a, b, c) __builtin_amdgcn_mfma_f32_32x32x16_bf16(a, b, c, 0, 0, 0)
#define SB() __builtin_amdgcn_sched_barrier(0)
#define FENCE() asm volatile("" ::: "memory")
#define PIN(x) asm volatile("" : "+v"(x))
#define LGKM0() do { FENCE(); __builtin_amdgcn_s_waitcnt(0xC07F); FENCE(); } while (0)
#define BAR() do { FENCE(); __builtin_amdgcn_s_waitcnt(0xC07F); __builtin_amdgcn_s_barrier(); FENCE(); } while (0)
#define VMW(n) asm volatile("s_waitcnt vmcnt(%0)" ::"i"(n) : "memory")
#define RLX_AGENT __ATOMIC_RELAXED, __HIP_MEMORY_SCOPE_AGENT
constexpr unsigned long long MASK56 = 0x00FFFFFFFFFFFFFFull;

template <int P>
__device__ __forceinline__ void dma4(const void* sbase, unsigned v0, unsigned v1, unsigned v2, unsigned v3, unsigned lds_dst) {
    asm volatile("s_mov_b32 m0, %5\n\ts_nop 0\n\tglobal_load_lds_dwordx4 %0, %4\n\tglobal_load_lds_dwordx4 %1, %4 offset:%6\n\t"
                 "global_load_lds_dwordx4 %2, %4 offset:%7\n\tglobal_load_lds_dwordx4 %3, %4 offset:%8"
                 ::"v"(v0), "v"(v1), "v"(v2), "v"(v3), "s"(sbase), "s"(lds_dst), "i"(P), "i"(2 * P), "i"(3 * P) : "memory");
}
template <int P>
__device__ __forceinline__ void dma4m(const void* sbase, unsigned v0, unsigned v1, unsigned v2, unsigned v3, unsigned lds_dst) {
    asm volatile("s_mov_b32 m0, %5\n\ts_mov_b64 exec, %9\n\tglobal_load_lds_dwordx4 %0, %4\n\tglobal_load_lds_dwordx4 %1, %4 offset:%6\n\t"
                 "global_load_lds_dwordx4 %2, %4 offset:%7\n\tglobal_load_lds_dwordx4 %3, %4 offset:%8\n\ts_mov_b64 exec, -1"
                 ::"v"(v0), "v"(v1), "v"(v2), "v"(v3), "s"(sbase), "s"(lds_dst), "i"(P), "i"(2 * P), "i"(3 * P), "s"(MASK56) : "memory");
}
template <int P>
__device__ __forceinline__ void dma3(const void* sbase, unsigned v0, unsigned v1, unsigned v2, unsigned lds_dst) {
    asm volatile("s_mov_b32 m0, %4\n\ts_nop 0\n\tglobal_load_lds_dwordx4 %0, %3\n\tglobal_load_lds_dwordx4 %1, %3 offset:%5\n\t"
                 "global_load_lds_dwordx4 %2, %3 offset:%6"
                 ::"v"(v0), "v"(v1), "v"(v2), "s"(sbase), "s"(lds_dst), "i"(P), "i"(2 * P) : "memory");
}
template <int P>
__device__ __forceinline__ void dma2m(const void* sbase, unsigned v0, unsigned v1, unsigned lds_dst) {
    asm volatile("s_mov_b32 m0, %3\n\ts_mov_b64 exec, %5\n\tglobal_load_lds_dwordx4 %0, %2\n\tglobal_load_lds_dwordx4 %1, %2 offset:%4\n\ts_mov_b64 exec, -1"
                 ::"v"(v0), "v"(v1), "s"(sbase), "s"(lds_dst), "i"(P), "s"(MASK56) : "memory");
}
__device__ __forceinline__ void dma1m(const void* sbase, unsigned v0, unsigned lds_dst) {
    asm volatile("s_mov_b32 m0, %2\n\ts_mov_b64 exec, %3\n\tglobal_load_lds_dwordx4 %0, %1\n\ts_mov_b64 exec, -1"
                 ::"v"(v0), "s"(sbase), "s"(lds_dst), "s"(MASK56) : "memory");
}
typedef __bf16 bf16x2_t __attribute__((ext_vector_type(2)));
typedef float f32x2 __attribute__((ext_vector_type(2)));
__device__ __forceinline__ unsigned cvtpk(float lo, float hi) {
    return __builtin_bit_cast(unsigned, __builtin_convertvector((f32x2){lo, hi}, bf16x2_t));
}
__device__ __forceinline__ s16x4 trread(lds_cptr p) {
    return __builtin_bit_cast(s16x4, __builtin_amdgcn_ds_read_tr16_b64_v4i16((LDSAS s16x4*)p));
}
__device__ __forceinline__ bf16x8 cat8(s16x4 a, s16x4 b) { return (bf16x8){a[0], a[1], a[2], a[3], b[0], b[1], b[2], b[3]}; }
__device__ __forceinline__ bf16x8 ldsread16(lds_cptr p) { return *(const LDSAS bf16x8*)p; }
__device__ __forceinline__ bf16x8 pack8(const f32x16& a, int b) {
    u32x4 w = {cvtpk(a[b], a[b + 1]), cvtpk(a[b + 2], a[b + 3]), cvtpk(a[b + 4], a[b + 5]), cvtpk(a[b + 6], a[b + 7])};
    return __builtin_bit_cast(bf16x8, w);
}
__device__ __forceinline__ int rfl(int x) { return __builtin_amdgcn_readfirstlane(x); }

constexpr unsigned WS_PQ = 0;
constexpr unsigned WS_O = 128u * 28 * 1024;
constexpr unsigned WS_DATA_BYTES = WS_O + 128u * 16 * 1024;
constexpr size_t WS_SYNC = (size_t)8 << 20;
constexpr int SY_ACKB = 0, SY_ACKA = 32 * 16, SY_REQB = SY_ACKA + 8 * 16, SY_REQA = SY_REQB + 128 * 16;
constexpr int LDS_TOTAL = 163840;
constexpr int LDS_SY = LDS_TOTAL - 256;
constexpr unsigned SPIN_CAP = 1u << 16;

struct Params {
    const u16* Ax; const u16* Wqkv; const u16* Wo; const u16* Bx; const u16* Cre; const u16* Cim; const u16* Wre; const u16* Wim;
    char* ws; u16* out;
};

__device__ __forceinline__ u64 sy_reg_load(const Params& P, int lane, int np, int ack_idx) {
    gu64* sy = (gu64*)(P.ws + WS_SYNC);
    return __hip_atomic_load(sy + ack_idx + (lane < np ? lane : 0), RLX_AGENT);
}
__device__ __forceinline__ void sy_reg_commit(const Params& P, char* lds, int lane, int np, int req_idx, u64 a) {
    gu64* sy = (gu64*)(P.ws + WS_SYNC);
    if (lane < np) {
        __hip_atomic_store(sy + req_idx + lane * 16, a + 1, RLX_AGENT);
        *(LDSAS u64*)((lds_ptr)lds + LDS_SY + lane * 8) = a + 1;
    }
}
__device__ __forceinline__ void touch(const void* p, char* lds) {
    asm volatile("s_mov_b32 m0, %1\n\ts_nop 0\n\tglobal_load_lds_dword %0, off" ::"v"(p), "s"((unsigned)(uintptr_t)lds + LDS_TOTAL - 512) : "memory");
}
struct SyPub { u64 a, r; };
__device__ __forceinline__ SyPub sy_pub_load(const Params& P, int lane, int nc, int ack_idx, int req_row) {
    gu64* sy = (gu64*)(P.ws + WS_SYNC);
    SyPub s;
    s.a = __hip_atomic_load(sy + ack_idx, RLX_AGENT);
    s.r = __hip_atomic_load(sy + req_row + (lane < nc ? lane : 0), RLX_AGENT);
    return s;
}
__device__ __forceinline__ void sy_publish(const Params& P, int lane, int nc, int ack_idx, int req_row, SyPub s) {
    gu64* sy = (gu64*)(P.ws + WS_SYNC);
    const u64 want = s.a + 1;
    u64 r = s.r;
    for (unsigned spins = 0; spins < SPIN_CAP; ++spins) {
        if (__all(r == want)) break;
        __builtin_amdgcn_s_sleep(2);
        r = __hip_atomic_load(sy + req_row + (lane < nc ? lane : 0), RLX_AGENT);
    }
    if (lane == 0) __hip_atomic_store(sy + ack_idx, want, RLX_AGENT);
}
__device__ __forceinline__ void sy_wait(const Params& P, char* lds, int lane, int np, int ack_idx) {
    gu64* sy = (gu64*)(P.ws + WS_SYNC);
    const u64 want = *(const LDSAS u64*)((lds_cptr)lds + LDS_SY + (lane & 15) * 8);
    for (unsigned spins = 0; spins < SPIN_CAP; ++spins) {
        bool ok = true;
        if (lane < np) ok = __hip_atomic_load(sy + ack_idx + lane, RLX_AGENT) == want;
        if (__all(ok)) break;
        __builtin_amdgcn_s_sleep(1);
    }
    FENCE();
}
__device__ __forceinline__ __amdgpu_buffer_rsrc_t ws_rsrc(const Params& P) {
    return __builtin_amdgcn_make_buffer_rsrc((void*)P.ws, 0, WS_DATA_BYTES, 0x00020000);
}
__device__ __forceinline__ void st_sc1(__amdgpu_buffer_rsrc_t rs, unsigned off, bf16x8 v) {
    __builtin_amdgcn_raw_buffer_store_b128(__builtin_bit_cast(u32x4, v), rs, off, 0, 16);
}
__device__ __forceinline__ bf16x8 ld_sc1(__amdgpu_buffer_rsrc_t rs, unsigned off) {
    return __builtin_bit_cast(bf16x8, __builtin_amdgcn_raw_buffer_load_b128(rs, off, 0, 16));
}

__device__ __forceinline__ void st_out(char* p, u32x4 v) {
    asm volatile("global_store_dwordx4 %0, %1, off sc1\n\ts_nop 1" ::"v"(p), "v"(v) : "memory");
}
constexpr int T_STG = 143360;

template <int NE>
struct TailB {
    static constexpr int SL = 2 * NE * 16 * 224;
    unsigned v[4]; const char* wb; unsigned wd;
    __device__ __forceinline__ void init(const Params& P, char* lds, int lane, int lw, int e0) {
        const int l = lane < 56 ? lane : 55;
#pragma unroll
        for (int i = 0; i < 4; ++i) v[i] = (4 * i + l / 14) * (D * 2) + (l % 14) * 16 + (3 - i) * 896;
        wb = (const char*)(((lw >> 1) ? P.Wim : P.Wre) + (size_t)(e0 + (lw & 1) * (NE * 8)) * D) - 3 * 896;
        wd = (unsigned)(uintptr_t)lds + lw * (NE * 2) * 896;
    }
    __device__ __forceinline__ void issue(int j, int pos) {
        const char* p = wb + j * 224; const unsigned d = wd + pos * SL;
        dma4m<896>(p, v[0], v[1], v[2], v[3], d);
        dma4m<896>(p + 16 * D * 2, v[0], v[1], v[2], v[3], d + 4 * 896);
        if (NE == 5) dma2m<896>(p + 32 * D * 2, v[0], v[1], d + 8 * 896);
    }
};
template <int NE>
__device__ __forceinline__ void tail_b_slice(f32x4* acc, const bf16x8* af, lds_cptr wr, lds_cptr wm) {
    bf16x8 wf[NE], wn[NE];
#pragma unroll
    for (int n = 0; n < NE; ++n) wf[n] = ldsread16(wr + n * 16 * 224);
#pragma unroll
    for (int s = 0; s < 7; ++s) {
        const int s1 = s + 1;
#pragma unroll
        for (int n = 0; n < NE; ++n) {
            acc[n] = MFMA16(wf[n], af[s], acc[n]); PIN(acc[n]); SB();
            if (s1 < 7) {
                wn[n] = (s1 < 3) ? ldsread16(wr + n * 16 * 224 + s1 * 64)
                      : (s1 < 6) ? ldsread16(wr + NE * 16 * 224 + n * 16 * 224 + (s1 - 3) * 64)
                                 : ldsread16(wm + n * 16 * 224);
                SB();
            }
        }
#pragma unroll
        for (int n = 0; n < NE; ++n) wf[n] = wn[n];
    }
}
template <int NE>
__device__ __forceinline__ void tail_b_store(const Params& P, char* lds, const f32x4* acc, int lane, int wid, int trow0, int e0) {
    const int fr = lane & 15, g = lane >> 4;
    constexpr int PIT = NE * 32 + 16;
    lds_ptr stg = (lds_ptr)lds + T_STG + wid * 4096;
#pragma unroll
    for (int n = 0; n < NE; ++n) {
        u32x2 w = {cvtpk(acc[n][0], acc[n][1]), cvtpk(acc[n][2], acc[n][3])};
        *(LDSAS u32x2*)(stg + fr * PIT + n * 32 + g * 8) = w;
    }
    LGKM0();
    u16* ob = P.out + (size_t)trow0 * 896 + 448 + e0;
#pragma unroll
    for (int i = 0; i < (16 * NE * 2 + 63) / 64; ++i) {
        const int e = i * 64 + lane;
        if (e < 16 * NE * 2) {
            const int row = e / (NE * 2), c = e % (NE * 2);
            st_out((char*)(ob + (size_t)row * 896) + c * 16, *(const LDSAS u32x4*)(stg + row * PIT + c * 16));
        }
    }
}
template <int NE>
__device__ __forceinline__ void tail_b_compute(const Params& P, char* lds, int lane, int wid, int tb, int e0) {
    constexpr int SL = TailB<NE>::SL;
    const int fr = lane & 15, g = lane >> 4;
    const __amdgpu_buffer_rsrc_t rs = ws_rsrc(P);
    const unsigned pq = WS_PQ + (unsigned)(tb * 4 + wid) * 28 * 1024 + lane * 16;
    bf16x8 af[28];
#pragma unroll
    for (int s = 0; s < 28; ++s) af[s] = ld_sc1(rs, pq + s * 1024);
    f32x4 acc[NE];
#pragma unroll
    for (int n = 0; n < NE; ++n) acc[n] = (f32x4){0, 0, 0, 0};
    lds_cptr wr = (lds_cptr)lds + fr * 224 + g * 16;
    lds_cptr wm = (lds_cptr)lds + (g >> 1) * (NE * 16 * 224) + fr * 224 + 192 + (g & 1) * 16;
#pragma unroll
    for (int j = 0; j < 4; ++j) tail_b_slice<NE>(acc, af + 7 * j, wr + j * SL, wm + j * SL);
    tail_b_store<NE>(P, lds, acc, lane, wid, tb * 64 + wid * 16, e0);
}

struct TailA {
    unsigned v[4]; const char* wb; unsigned wd;
    __device__ __forceinline__ void init(const Params& P, char* lds, int lane, int lw, int n0) {
        const int l = lane < 56 ? lane : 55;
#pragma unroll
        for (int i = 0; i < 4; ++i) v[i] = (4 * i + l / 14) * (D * 2) + (l % 14) * 16 + (3 - i) * 896;
        wb = (const char*)(P.Wo + (size_t)(lw * 28) * D + n0) - 3 * 896;
        wd = (unsigned)(uintptr_t)lds + lw * 7 * 896;
    }
    __device__ __forceinline__ void issue(int j) {
        const char* p = wb + (size_t)j * (112 * D * 2); const unsigned d = wd + j * (112 * 224);
        dma4m<896>(p, v[0], v[1], v[2], v[3], d);
        dma2m<896>(p + 16 * D * 2, v[0], v[1], d + 4 * 896);
        dma1m(p + 12 * D * 2 + 3 * 896, v[3], d + 6 * 896);
    }
};
__device__ __forceinline__ void tail_a_compute(const Params& P, char* lds, int lane, int wid, int tq, int n0) {
    const int fr = lane & 15, g = lane >> 4;
    const __amdgpu_buffer_rsrc_t rs = ws_rsrc(P);
    const unsigned of = WS_O + (unsigned)(tq * 8 + wid * 2) * 16 * 1024 + lane * 16;
    bf16x8 a0[16], a1[16];
#pragma unroll
    for (int s = 0; s < 16; ++s) { a0[s] = ld_sc1(rs, of + s * 1024); a1[s] = ld_sc1(rs, of + 16384 + s * 1024); }
    f32x4 acc0[7], acc1[7];
#pragma unroll
    for (int n = 0; n < 7; ++n) { acc0[n] = (f32x4){0, 0, 0, 0}; acc1[n] = (f32x4){0, 0, 0, 0}; }
    const int q = fr >> 2, p4 = lane & 3;
    const int d0 = (g == 0) ? 0 : (g == 1) ? 4 : (g == 2) ? 16 : 20;
    const int d1 = (g == 0) ? 8 : (g == 1) ? 12 : 24;
    lds_cptr r0 = (lds_cptr)lds + (d0 + q) * 224 + p4 * 8;
    lds_cptr r1 = (lds_cptr)lds + (d1 + q) * 224 + p4 * 8;
    s16x4 lo[7], hi[7], ln[7], hn[7];
#pragma unroll
    for (int n = 0; n < 7; ++n) { lo[n] = trread(r0 + n * 32); hi[n] = trread(r1 + n * 32); }
#pragma unroll
    for (int h = 0; h < 16; ++h) {
#pragma unroll
        for (int n = 0; n < 7; ++n) {
            const bf16x8 wf = cat8(lo[n], hi[n]);
            acc0[n] = MFMA16(wf, a0[h], acc0[n]); PIN(acc0[n]); SB();
            if (h < 15) { ln[n] = trread(r0 + (h + 1) * (28 * 224) + n * 32); SB(); }
            acc1[n] = MFMA16(wf, a1[h], acc1[n]); PIN(acc1[n]); SB();
            if (h < 15) { hn[n] = trread(r1 + (h + 1) * (28 * 224) + n * 32); SB(); }
        }
        if (h < 15) {
#pragma unroll
            for (int n = 0; n < 7; ++n) { lo[n] = ln[n]; hi[n] = hn[n]; }
        }
    }
    constexpr int PIT = 240;
    lds_ptr stg = (lds_ptr)lds + 100352 + wid * 8192;
#pragma unroll
    for (int n = 0; n < 7; ++n) {
        u32x2 w0 = {cvtpk(acc0[n][0], acc0[n][1]), cvtpk(acc0[n][2], acc0[n][3])};
        u32x2 w1 = {cvtpk(acc1[n][0], acc1[n][1]), cvtpk(acc1[n][2], acc1[n][3])};
        *(LDSAS u32x2*)(stg + fr * PIT + n * 32 + g * 8) = w0;
        *(LDSAS u32x2*)(stg + (16 + fr) * PIT + n * 32 + g * 8) = w1;
    }
    LGKM0();
    u16* ob = P.out + (size_t)(tq * 128 + wid * 32) * 896 + n0;
#pragma unroll
    for (int i = 0; i < 7; ++i) {
        const int e = i * 64 + lane;
        const int row = e / 14, c = e % 14;
        st_out((char*)(ob + (size_t)row * 896) + c * 16, *(const LDSAS u32x4*)(stg + row * PIT + c * 16));
    }
}

constexpr int B_CT = 16384, B_XP = 960, B_XT = 16 * B_XP, B_STAGE = B_CT + B_XT;
constexpr int B_NS = 5;
constexpr int B_RED = 0;

__device__ __forceinline__ void role_b(const Params& P, char* lds, int kx, int ii, int dj) {
    const int tid = threadIdx.x, lane = tid & 63;
    const int wid = rfl(tid >> 6);
    const unsigned lds0 = (unsigned)(uintptr_t)lds;
    const int tb = 4 * kx + ii, t0 = tb * 64, rot = (ii + dj) & 3;
    const int e0 = 80 * dj;
    if (wid >= 4) {
        const int lw = wid - 4;
        unsigned cv[4], xv[4];
        {
            const int r = lane >> 3, c = lane & 7, l = lane < 56 ? lane : 55;
#pragma unroll
            for (int i = 0; i < 4; ++i) {
                cv[i] = (8 * i + r) * (S * 2) + ((c ^ ((4 * i + (r >> 1)) & 7)) * 16) + (3 - i) * 1024;
                xv[i] = (4 * i + l / 14) * (D * 2) + (l % 14) * 16 + (3 - i) * B_XP;
            }
        }
        const char* cb0 = (const char*)((lw < 2 ? P.Cre : P.Cim) + (size_t)(t0 + (lw & 1) * 32) * S) - 3 * 1024;
        const char* xb0 = (const char*)(P.Bx + (size_t)(lw * 16) * D + dj * 112) - 3 * B_XP;
        const unsigned cd = lds0 + lw * 4096, xd = lds0 + B_CT + lw * 4 * B_XP;
        const int ph = 2 * (kx & 3);
        auto issue = [&](int s, int slot) {
            int base, L;
            if (s < ph) { base = 0; L = ph; } else if (s < ph + 24) { base = ph + ((s - ph) & ~7); L = 8; } else { base = ph + 24; L = 8 - ph; }
            int ks = (s - base) + ((rot * L) >> 2); if (ks >= L) ks -= L; ks += base;
            const unsigned so = slot * B_STAGE;
            dma4<1024>(cb0 + ks * 128, cv[0], cv[1], cv[2], cv[3], cd + so);
            dma4m<B_XP>(xb0 + (size_t)ks * (64 * D * 2), xv[0], xv[1], xv[2], xv[3], xd + so);
        };
        issue(0, 0); issue(1, 1);
        VMW(8); BAR();
        issue(2, 2); issue(3, 3);
        int sl = 4;
        for (int p = 0; p < 14; ++p) {
            VMW(8);
            BAR();
            issue(2 * p + 4, sl); sl = (sl == B_NS - 1) ? 0 : sl + 1;
            issue(2 * p + 5, sl); sl = (sl == B_NS - 1) ? 0 : sl + 1;
        }
        TailB<5> tw; tw.init(P, lds, lane, lw, e0);
        VMW(8); BAR();
        tw.issue(dj, 2);
        VMW(10); BAR();
        tw.issue((dj + 1) & 3, 3);
        BAR();
        BAR();
        VMW(0);
        BAR();
        tw.issue((dj + 2) & 3, 0); tw.issue((dj + 3) & 3, 1);
        VMW(0);
        BAR();
        return;
    }
    u64 sy_a = 0;
    if (wid == 0) sy_a = sy_reg_load(P, lane, 4, SY_ACKB + tb * 16);
    const int tw = wid & 1, kh = wid >> 1;
    const int fr = lane & 15, g = lane >> 4;
    const int crow = 32 * tw + fr;
    const int coff = crow * 128 + (((kh * 4 + g) ^ ((crow >> 1) & 7)) * 16);
    const int q = fr >> 2, p4 = lane & 3;
    const int xr0 = kh * 32 + 8 * g + q;
    const int xoff0 = B_CT + (xr0 >> 2) * B_XP + (xr0 & 3) * 224 + p4 * 8;
    f32x4 acc[4][7];
#pragma unroll
    for (int m = 0; m < 4; ++m)
#pragma unroll
        for (int n = 0; n < 7; ++n) acc[m][n] = (f32x4){0, 0, 0, 0};
    bf16x8 cfA[4], xfA[7], cfB[4], xfB[7];
#define MM(cf, xf, m, n) do { acc[m][n] = MFMA16(xf[n], cf[m], acc[m][n]); PIN(acc[m][n]); } while (0)
#define RL(t, st_, n) t = trread(st_ + xoff0 + (n) * 32)
#define RH(t, st_, n) t = trread(st_ + xoff0 + B_XP + (n) * 32)
#define RC(d, st_, m) d = ldsread16(st_ + coff + ((m) & 1) * 2048 + ((m) >> 1) * 8192)
#define G4(cf, xf, n, R0, R1, R2) do { MM(cf, xf, 0, n); SB(); R0; SB(); MM(cf, xf, 1, n); SB(); R1; SB(); MM(cf, xf, 2, n); SB(); R2; SB(); MM(cf, xf, 3, n); SB(); } while (0)
#define NOP_ do {} while (0)
#define B_STEP(cf, xf, cn, xn, st_) do { s16x4 lo[7], hi[7]; \
        G4(cf, xf, 0, RC(cn[0], st_, 0), RC(cn[1], st_, 1), RC(cn[2], st_, 2)); \
        G4(cf, xf, 1, RC(cn[3], st_, 3), RL(lo[0], st_, 0), RH(hi[0], st_, 0)); \
        G4(cf, xf, 2, RL(lo[1], st_, 1), RH(hi[1], st_, 1), RL(lo[2], st_, 2)); \
        G4(cf, xf, 3, RH(hi[2], st_, 2), RL(lo[3], st_, 3), RH(hi[3], st_, 3)); \
        G4(cf, xf, 4, RL(lo[4], st_, 4), RH(hi[4], st_, 4), RL(lo[5], st_, 5)); \
        G4(cf, xf, 5, RH(hi[5], st_, 5), RL(lo[6], st_, 6), RH(hi[6], st_, 6)); \
        G4(cf, xf, 6, NOP_, NOP_, NOP_); \
        _Pragma("unroll") for (int n_ = 0; n_ < 7; ++n_) xn[n_] = cat8(lo[n_], hi[n_]); } while (0)
    BAR();
    {
        lds_cptr st_ = (lds_cptr)lds; s16x4 lo[7], hi[7];
        RC(cfA[0], st_, 0); RC(cfA[1], st_, 1); RC(cfA[2], st_, 2); RC(cfA[3], st_, 3);
#pragma unroll
        for (int n = 0; n < 7; ++n) { RL(lo[n], st_, n); RH(hi[n], st_, n); xfA[n] = cat8(lo[n], hi[n]); }
    }
    int sl = 1;
    for (int p = 0; p < 15; ++p) {
        if (p == 1 && wid == 0) sy_reg_commit(P, lds, lane, 4, SY_REQB + (tb * 4) * 16 + dj, sy_a);
        BAR(); SB();
        { lds_cptr st_ = (lds_cptr)lds + sl * B_STAGE; B_STEP(cfA, xfA, cfB, xfB, st_); sl = (sl == B_NS - 1) ? 0 : sl + 1; }
        { lds_cptr st_ = (lds_cptr)lds + sl * B_STAGE; B_STEP(cfB, xfB, cfA, xfA, st_); sl = (sl == B_NS - 1) ? 0 : sl + 1; }
    }
    BAR(); SB();
    { lds_cptr st_ = (lds_cptr)lds + sl * B_STAGE; B_STEP(cfA, xfA, cfB, xfB, st_); }
#pragma unroll
    for (int n = 0; n < 7; ++n) { MM(cfB, xfB, 0, n); MM(cfB, xfB, 1, n); MM(cfB, xfB, 2, n); MM(cfB, xfB, 3, n); }
    BAR();
    SyPub sp = {0, 0};
    if (wid == 0) sp = sy_pub_load(P, lane, 6, SY_ACKB + tb * 16 + dj, SY_REQB + (tb * 4 + dj) * 16);
    LDSAS f32x4* red = (LDSAS f32x4*)((lds_ptr)lds + B_RED) + lane;
    if (kh == 0) {
#pragma unroll
        for (int n = 0; n < 7; ++n) { red[((tw * 2 + 0) * 14 + n) * 64] = acc[1][n]; red[((tw * 2 + 0) * 14 + 7 + n) * 64] = acc[3][n]; }
    } else {
#pragma unroll
        for (int n = 0; n < 7; ++n) { red[((tw * 2 + 1) * 14 + n) * 64] = acc[0][n]; red[((tw * 2 + 1) * 14 + 7 + n) * 64] = acc[2][n]; }
    }
    BAR();
    lds_ptr img = (lds_ptr)lds + B_RED + (tw * 2 + (1 - kh)) * 14336;
    {
        f32x4 t[14];
        const LDSAS f32x4* rd = (const LDSAS f32x4*)img + lane;
#pragma unroll
        for (int n = 0; n < 14; ++n) t[n] = rd[n * 64];
        LGKM0();
#define FIN(MP_, MQ_) do { _Pragma("unroll") for (int n = 0; n < 7; ++n) { \
            const f32x4 a = acc[MP_][n] + t[n], b = -acc[MQ_][n] - t[7 + n]; \
            u32x2 wp = {cvtpk(a[0], a[1]), cvtpk(a[2], a[3])}, wq = {cvtpk(b[0], b[1]), cvtpk(b[2], b[3])}; \
            *(LDSAS u32x2*)(img + fr * 240 + (16 * n + 4 * g) * 2) = wp; \
            *(LDSAS u32x2*)(img + 3840 + fr * 240 + (16 * n + 4 * g) * 2) = wq; } } while (0)
        if (kh == 0) FIN(0, 2); else FIN(1, 3);
#undef FIN
    }
    LGKM0();
    const __amdgpu_buffer_rsrc_t rs = ws_rsrc(P);
    const int t16 = tb * 4 + tw * 2 + kh;
    bf16x8 own[7];
    {
        const unsigned dst = WS_PQ + ((unsigned)t16 * 28 + dj * 7) * 1024 + lane * 16;
        lds_cptr rp = (lds_cptr)img + fr * 240 + g * 16;
#pragma unroll
        for (int i = 0; i < 3; ++i) { own[i] = ldsread16(rp + i * 64); own[3 + i] = ldsread16(rp + 3840 + i * 64); }
        own[6] = ldsread16((lds_cptr)img + (g >> 1) * 3840 + fr * 240 + 192 + (g & 1) * 16);
#pragma unroll
        for (int i = 0; i < 7; ++i) st_sc1(rs, dst + i * 1024, own[i]);
    }
    VMW(0);
    BAR();
    if (wid == 0) sy_publish(P, lane, 6, SY_ACKB + tb * 16 + dj, SY_REQB + (tb * 4 + dj) * 16, sp);
    {
        constexpr int SL = TailB<5>::SL;
        f32x4 ta[5];
#pragma unroll
        for (int n = 0; n < 5; ++n) ta[n] = (f32x4){0, 0, 0, 0};
        lds_cptr wr = (lds_cptr)lds + fr * 224 + g * 16;
        lds_cptr wm = (lds_cptr)lds + (g >> 1) * (5 * 16 * 224) + fr * 224 + 192 + (g & 1) * 16;
        tail_b_slice<5>(ta, own, wr + 2 * SL, wm + 2 * SL);
        if (wid == 0) sy_wait(P, lds, lane, 4, SY_ACKB + tb * 16);
        BAR();
        bf16x8 af[21];
#pragma unroll
        for (int q2 = 1; q2 < 4; ++q2) {
            const unsigned src = WS_PQ + ((unsigned)t16 * 28 + ((dj + q2) & 3) * 7) * 1024 + lane * 16;
#pragma unroll
            for (int s = 0; s < 7; ++s) af[(q2 - 1) * 7 + s] = ld_sc1(rs, src + s * 1024);
        }
        tail_b_slice<5>(ta, af, wr + 3 * SL, wm + 3 * SL);
        tail_b_slice<5>(ta, af + 7, wr, wm);
        tail_b_slice<5>(ta, af + 14, wr + SL, wm + SL);
        tail_b_store<5>(P, lds, ta, lane, wid, t16 * 16, e0);
    }
#undef MM
#undef RL
#undef RH
#undef RC
#undef G4
#undef B_STEP
}

constexpr int A_XS = 32768;
constexpr int A_WOFF = 4 * A_XS;
constexpr int A_WS = 12288;
constexpr int A_KV = 65536, A_Q = 0;

__device__ __forceinline__ void role_a(const Params& P, char* lds, int qb, int h) {
    const int tid = threadIdx.x, lane = tid & 63;
    const int wid = rfl(tid >> 6);
    const unsigned lds0 = (unsigned)(uintptr_t)lds;
    const int q0 = qb * 256;
    const int rot = h % 7;
    const int r32 = lane & 31, hi = lane >> 5;
    const bool tailA = h < 8;
    const int tq = 2 * qb + (h >> 2), n0 = 112 * (h & 3);
    const int tb = 4 * qb + ((h - 8) >> 1), eg = 4 + (h & 1), e0 = 320 + 64 * (h & 1);
    if (wid >= 4) {
        const int lw = wid - 4;
        unsigned xv[4], wv[3];
        {
            const int r = lane >> 3, c = lane & 7;
#pragma unroll
            for (int i = 0; i < 4; ++i) xv[i] = (8 * i + r) * (D * 2) + ((c ^ ((4 * i + (r >> 1)) & 7)) * 16) + (3 - i) * 1024;
            const int c4 = lane & 3, kr = lane >> 2;
#pragma unroll
            for (int sec = 0; sec < 3; ++sec) wv[sec] = (kr * NQKV + sec * D + (c4 < 3 ? 8 * c4 : 20)) * 2 + (2 - sec) * 1024;
        }
        const int rown = q0 + 64 * lw, rprev = (qb > 0) ? rown - 256 : rown;
        const char* xbp = (const char*)(P.Ax + (size_t)rprev * D) - 3 * 1024;
        const char* xbo = (const char*)(P.Ax + (size_t)rown * D) - 3 * 1024;
        const char* wb = (const char*)(P.Wqkv + (size_t)(16 * lw) * NQKV + h * DH) - 2 * 1024;
        auto kcof = [&](int c) { int k = c + rot; return k >= 7 ? k - 7 : k; };
        auto issue_x = [&](int j) {
            const int off = kcof(j >> 1) * 128 + (j & 1) * (32 * D * 2);
            const unsigned dst = lds0 + (j & 3) * A_XS + lw * 8192;
            dma4<1024>(xbp + off, xv[0], xv[1], xv[2], xv[3], dst);
            dma4<1024>(xbo + off, xv[0], xv[1], xv[2], xv[3], dst + 4096);
        };
        auto issue_w = [&](int c) {
            dma3<1024>(wb + (size_t)kcof(c) * (64 * NQKV * 2), wv[0], wv[1], wv[2], lds0 + A_WOFF + (c & 1) * A_WS + lw * 3072);
        };
        issue_w(0); issue_x(0); issue_x(1);
        VMW(8);
        BAR();
        VMW(0);
        BAR();
        issue_w(1); issue_x(2); issue_x(3);
        for (int j = 1; j < 11; j += 2) {
            VMW(8); BAR();
            issue_x(j + 3); issue_w((j + 3) >> 1);
            VMW(11); BAR();
            issue_x(j + 4);
        }
        VMW(8); BAR();
        VMW(0); BAR();
        BAR();
    } else {
        u64 sy_a = 0;
        if (wid == 0) sy_a = sy_reg_load(P, lane, tailA ? 16 : 4, tailA ? SY_ACKA + qb * 16 : SY_ACKB + tb * 16);
        lds_cptr xb = (lds_cptr)lds + wid * 8192 + r32 * 128;
        const int xf = (r32 >> 1) & 7;
        int xo4[4];
#pragma unroll
        for (int ks = 0; ks < 4; ++ks) xo4[ks] = ((2 * ks + hi) ^ xf) * 16;
        const int wq_ = (lane & 15) >> 2, wp = lane & 3, wc = (lane >> 4) & 1;
        lds_cptr wb = (lds_cptr)lds + A_WOFF + (8 * hi + wq_) * 64 + wc * 32 + wp * 8;
        f32x16 aQ0 = {}, aKo0 = {}, aVo0 = {}, aKp0 = {}, aVp0 = {}, aQ1 = {}, aKo1 = {}, aVo1 = {}, aKp1 = {}, aVp1 = {};
        bf16x8 fq[4], fk[4], fv[4], xpA, xoA, xpB, xoB;
        s16x4 wl, wh;
#define RXP(d, j, ks) d = ldsread16(xb + ((j) & 3) * A_XS + xo4[ks])
#define RXO(d, j, ks) d = ldsread16(xb + ((j) & 3) * A_XS + 4096 + xo4[ks])
#define RW(d, c, ks, sec) do { lds_cptr w_ = wb + ((c) & 1) * A_WS + (ks) * 3072 + (sec) * 1024; wl = trread(w_); wh = trread(w_ + 256); d = cat8(wl, wh); } while (0)
#define M32(acc, a, b) do { acc = MFMA32(a, b, acc); PIN(acc); SB(); } while (0)
#define A_KS(H, ks, xp, xo, xpn, xon, jn, kn, LX, LW, cn, wn) do { \
            M32(aQ##H, fq[ks], xo); if (LX) { RXP(xpn, jn, kn); } if (LW) { RW(fq[wn], cn, wn, 0); } SB(); \
            M32(aKo##H, fk[ks], xo); if (LX) { RXO(xon, jn, kn); } SB(); \
            M32(aVo##H, xo, fv[ks]); if (LW) { RW(fk[wn], cn, wn, 1); } SB(); \
            M32(aKp##H, fk[ks], xp); if (LW) { RW(fv[wn], cn, wn, 2); } SB(); \
            M32(aVp##H, xp, fv[ks]); } while (0)
        BAR();
        RXP(xpA, 0, 0); RXO(xoA, 0, 0); RW(fq[0], 0, 0, 0); RW(fk[0], 0, 0, 1); RW(fv[0], 0, 0, 2);
        const bf16x8 ones = {0x3F80, 0x3F80, 0x3F80, 0x3F80, 0x3F80, 0x3F80, 0x3F80, 0x3F80};
        const bool oc = (r32 == 24);
        LDSAS bf16x8* kv = (LDSAS bf16x8*)((lds_ptr)lds + A_KV + lane * 16);
        LDSAS bf16x8* qd = (LDSAS bf16x8*)((lds_ptr)lds + A_Q + lane * 16);
#define PUTKV(tile, K, V) do { LDSAS bf16x8* d_ = kv + (tile) * 256; d_[0] = pack8(K, 0); d_[64] = pack8(K, 8); \
            d_[128] = oc ? ones : pack8(V, 0); d_[192] = oc ? ones : pack8(V, 8); } while (0)
#define PUTQ(tile, Q) do { if (hi == 0) { Q[12] = 0.f; Q[13] = 0.f; Q[14] = 0.f; Q[15] = 0.f; } \
            qd[(tile) * 128] = pack8(Q, 0); qd[(tile) * 128 + 64] = pack8(Q, 8); } while (0)
        for (int c = 0; c < 7; ++c) {
            const int j = 2 * c;
            if (c == 1 && wid == 0) sy_reg_commit(P, lds, lane, tailA ? 16 : 4, tailA ? SY_REQA + (qb * 16) * 16 + (h & 7) : SY_REQB + (tb * 4) * 16 + eg, sy_a);
            A_KS(0, 0, xpA, xoA, xpB, xoB, j, 1, true, true, c, 1);
            A_KS(0, 1, xpB, xoB, xpA, xoA, j, 2, true, true, c, 2);
            A_KS(0, 2, xpA, xoA, xpB, xoB, j, 3, true, true, c, 3);
            if (c == 0) BAR();
            A_KS(0, 3, xpB, xoB, xpA, xoA, j + 1, 0, true, false, c, 0);
            BAR();
            if (c < 6) {
                A_KS(1, 0, xpA, xoA, xpB, xoB, j + 1, 1, true, false, c, 0);
                A_KS(1, 1, xpB, xoB, xpA, xoA, j + 1, 2, true, false, c, 0);
                A_KS(1, 2, xpA, xoA, xpB, xoB, j + 1, 3, true, false, c, 0);
                A_KS(1, 3, xpB, xoB, xpA, xoA, j + 2, 0, true, true, c + 1, 0);
                BAR();
            }
        }
        A_KS(1, 0, xpA, xoA, xpB, xoB, 13, 1, true, false, 6, 0);
        PUTKV(2 * wid, aKp0, aVp0); SB();
        A_KS(1, 1, xpB, xoB, xpA, xoA, 13, 2, true, false, 6, 0);
        PUTKV(8 + 2 * wid, aKo0, aVo0); SB();
        A_KS(1, 2, xpA, xoA, xpB, xoB, 13, 3, true, false, 6, 0);
        PUTQ(2 * wid, aQ0); SB();
        A_KS(1, 3, xpB, xoB, xpA, xoA, 14, 0, false, false, 7, 0);
        PUTKV(2 * wid + 1, aKp1, aVp1); PUTKV(8 + 2 * wid + 1, aKo1, aVo1); PUTQ(2 * wid + 1, aQ1);
#undef PUTKV
#undef PUTQ
#undef RXP
#undef RXO
#undef RW
#undef M32
#undef A_KS
    }
    BAR();
    if (wid >= 4) {
#pragma unroll
        for (int i = 0; i < 3; ++i) {
            int ln = (h * 4 + (wid - 4)) * 49 + lane;
            if (lane >= 49) ln = (h * 4 + (wid - 4)) * 49;
            const u16* m = (i == 0) ? P.Wo : (i == 1) ? P.Wre : P.Wim;
            touch((const char*)m + ln * 128, lds);
        }
    }
    lds_cptr kvb = (lds_cptr)lds + A_KV + wid * 4096 + lane * 16;
    const bf16x8 qf0 = ldsread16((lds_cptr)lds + A_Q + wid * 2048 + lane * 16), qf1 = ldsread16((lds_cptr)lds + A_Q + wid * 2048 + 1024 + lane * 16);
    f32x16 sc[9];
    const float NEG = -__builtin_inff();
    if (qb > 0) {
        bf16x8 ka = ldsread16(kvb), kb = ldsread16(kvb + 1024);
#pragma unroll
        for (int j = 0; j < 9; ++j) {
            bf16x8 na, nb;
            if (j < 8) { na = ldsread16(kvb + (j + 1) * 4096); nb = ldsread16(kvb + (j + 1) * 4096 + 1024); }
            SB();
            f32x16 z = {};
            z = MFMA32(ka, qf0, z);
            sc[j] = MFMA32(kb, qf1, z);
            PIN(sc[j]);
            SB();
            if (j < 8) { ka = na; kb = nb; }
        }
    } else {
        f32x16 neg;
#pragma unroll
        for (int r = 0; r < 16; ++r) neg[r] = NEG;
        PIN(neg);
#pragma unroll
        for (int j = 0; j < 9; ++j) {
            if (j + wid >= 8) {
                f32x16 z = {};
                z = MFMA32(ldsread16(kvb + j * 4096), qf0, z);
                sc[j] = MFMA32(ldsread16(kvb + j * 4096 + 1024), qf1, z);
            } else sc[j] = neg;
            PIN(sc[j]);
        }
    }
#pragma unroll
    for (int r = 0; r < 16; ++r) {
        const int kk = (r & 3) + 8 * (r >> 2) + 4 * hi;
        if (kk <= r32) sc[0][r] = NEG;
        if (kk > r32) sc[8][r] = NEG;
    }
    float m0 = sc[8][0], m1 = sc[8][1];
#pragma unroll
    for (int j = 0; j < 9; ++j)
#pragma unroll
        for (int r = 0; r < 16; r += 4) {
            m0 = fmaxf(fmaxf(m0, sc[j][r]), sc[j][r + 1]);
            m1 = fmaxf(fmaxf(m1, sc[j][r + 2]), sc[j][r + 3]);
        }
    float m = fmaxf(m0, m1);
    {
        auto rr = __builtin_amdgcn_permlane32_swap(__float_as_uint(m), __float_as_uint(m), false, false);
        m = fmaxf(__uint_as_float(rr[0]), __uint_as_float(rr[1]));
    }
    SyPub sp = {0, 0};
    if (wid == 0) sp = sy_pub_load(P, lane, 8, SY_ACKA + qb * 16 + h, SY_REQA + (qb * 16 + h) * 16);
    constexpr float C2 = 0.18898223650461362f * 1.4426950408889634f;
    const float mc = -m * C2;
    f32x16 o = {};
#pragma unroll
    for (int j = 0; j < 9; ++j) {
        const bf16x8 v0 = ldsread16(kvb + j * 4096 + 2048), v1 = ldsread16(kvb + j * 4096 + 3072);
#pragma unroll
        for (int r = 0; r < 16; ++r) sc[j][r] = __builtin_amdgcn_exp2f(fmaf(sc[j][r], C2, mc));
        const bf16x8 p0 = pack8(sc[j], 0), p1 = pack8(sc[j], 8);
        o = MFMA32(v0, p0, o);
        o = MFMA32(v1, p1, o);
        PIN(o);
        SB();
    }
    float l = o[12];
    {
        auto rr = __builtin_amdgcn_permlane32_swap(__float_as_uint(l), __float_as_uint(l), false, false);
        l = __uint_as_float(rr[0]);
    }
    const float inv = 1.0f / l;
#pragma unroll
    for (int r = 0; r < 16; ++r) o[r] *= inv;
    if (hi == 0) { o[12] = 0.f; o[13] = 0.f; o[14] = 0.f; o[15] = 0.f; }
    {
        const __amdgpu_buffer_rsrc_t rs = ws_rsrc(P);
        const int t16 = (q0 + 32 * wid) / 16 + (r32 >> 4);
        const unsigned dst = WS_O + ((unsigned)t16 * 16 + h) * 1024 + hi * 256 + (r32 & 15) * 16;
        st_sc1(rs, dst, pack8(o, 0));
        st_sc1(rs, dst + 512, pack8(o, 8));
    }
    VMW(0);
    BAR();
    if (wid >= 4) {
        const int lw = wid - 4;
        if (tailA) { TailA tw; tw.init(P, lds, lane, lw, n0); tw.issue(0); tw.issue(1); tw.issue(2); tw.issue(3); }
        else { TailB<4> tw; tw.init(P, lds, lane, lw, e0); tw.issue(0, 0); tw.issue(1, 1); tw.issue(2, 2); tw.issue(3, 3); }
        VMW(0);
        BAR();
        return;
    }
    if (wid == 0) {
        sy_publish(P, lane, 8, SY_ACKA + qb * 16 + h, SY_REQA + (qb * 16 + h) * 16, sp);
        if (tailA) sy_wait(P, lds, lane, 16, SY_ACKA + qb * 16); else sy_wait(P, lds, lane, 4, SY_ACKB + tb * 16);
    }
    BAR();
    if (tailA) tail_a_compute(P, lds, lane, wid, tq, n0); else tail_b_compute<4>(P, lds, lane, wid, tb, e0);
}

__global__ __launch_bounds__(512, 2) void fused_kernel(Params P) {
    extern __shared__ __attribute__((aligned(16))) char lds[];
    const int b = blockIdx.x, k = b & 7, idx = b >> 3;
    if (idx < 16) role_b(P, lds, k, idx >> 2, idx & 3);
    else role_a(P, lds, k, idx - 16);
}

extern "C" void kernel_launch(void* const* d_in, const int* in_sizes, int n_in, void* d_out, int out_size, void* d_ws,
                              size_t ws_size, hipStream_t stream) {
    Params P;
    P.Ax = (const u16*)d_in[0]; P.Wqkv = (const u16*)d_in[1]; P.Wo = (const u16*)d_in[2]; P.Bx = (const u16*)d_in[3];
    P.Cre = (const u16*)d_in[4]; P.Cim = (const u16*)d_in[5]; P.Wre = (const u16*)d_in[6]; P.Wim = (const u16*)d_in[7];
    P.ws = (char*)d_ws; P.out = (u16*)d_out;
    (void)hipFuncSetAttribute((const void*)fused_kernel, hipFuncAttributeMaxDynamicSharedMemorySize, LDS_TOTAL);
    fused_kernel<<<256, 512, LDS_TOTAL, stream>>>(P);
}
```
